```python
import torch

def _softmax(x, axis=-1):
    return torch.softmax(x, dim=axis)

def _gelu(x):
    return torch.nn.functional.gelu(x, approximate='tanh')

def _silu(x):
    return torch.nn.functional.silu(x)

def _sigmoid(x):
    return torch.sigmoid(x)

def _layernorm(x, axis=-1, eps=1e-05):
    m = x.mean(dim=axis, keepdim=True)
    v = x.var(dim=axis, keepdim=True, unbiased=False)
    return (x - m) / torch.sqrt(v + eps)

def _scatter_add(tgt, idx, src, axis=0):
    return tgt.clone().index_add_(axis, idx, src)

def _segment_sum(src, seg, n):
    out = torch.zeros((n,) + src.shape[1:], dtype=src.dtype)
    return out.index_add_(0, seg, src)

def _pad(x, pw):
    flat = tuple((v for pair in reversed(pw) for v in pair))
    return torch.nn.functional.pad(x, flat)
kernel_name = 'adaLN_cond__mha_block_cached__geglu_ffn__decode'

def setup_inputs():

    _w = lambda a: (lambda t: t.to(torch.bfloat16) if t.is_floating_point() else t)(torch.as_tensor(a))

    def _A_setup():
        B, Sq, D = (4, 1, 512)
        H, Sk, Dh = (32, 4096, 128)
        return {'x': _w((lambda *s: torch.randn(*s, dtype=torch.bfloat16) * 0.1)(B, Sq, D)), 'k_cache': _w((lambda *s: torch.randn(*s, dtype=torch.bfloat16) * 0.8)(B, H, Sk, Dh)), 'v_cache': _w((lambda *s: torch.randn(*s, dtype=torch.bfloat16) * 0.8)(B, H, Sk, Dh)), 'wq': _w((lambda *s: torch.randn(*s, dtype=torch.bfloat16) * 0.8)(D, H * Dh) * D ** (-0.5)), 'wo': _w((lambda *s: torch.randn(*s, dtype=torch.bfloat16) * 0.8)(H * Dh, D) * (H * Dh) ** (-0.5))}
    _A_in = _A_setup()

    def _B_setup():
        B, S, D, F = (4, 1, 512, 1792)
        return {'x': _w((lambda *s: torch.randn(*s, dtype=torch.bfloat16) * 0.1)(B, S, D)), 'w_gate': _w((lambda *s: torch.randn(*s, dtype=torch.bfloat16) * 0.1)(D, F)), 'w_up': _w((lambda *s: torch.randn(*s, dtype=torch.bfloat16) * 0.1)(D, F)), 'w_down': _w((lambda *s: torch.randn(*s, dtype=torch.bfloat16) * 0.1)(F, D))}
    _B_in = _B_setup()
    return {'A_x': _A_in['x'], 'A_k_cache': _A_in['k_cache'], 'A_v_cache': _A_in['v_cache'], 'A_wq': _A_in['wq'], 'A_wo': _A_in['wo'], 'B_x': _B_in['x'], 'B_w_gate': _B_in['w_gate'], 'B_w_up': _B_in['w_up'], 'B_w_down': _B_in['w_down'], 'c': _w((lambda *s: torch.randn(*s, dtype=torch.bfloat16) * 0.8)(4, 512) * 8.0), 'w_mod': _w((lambda *s: torch.randn(*s, dtype=torch.bfloat16) * 0.8)(512, 6 * 512) * 512 ** (-0.5) * 8.0)}

def reference(A_x, A_k_cache, A_v_cache, A_wq, A_wo, B_x, B_w_gate, B_w_up, B_w_down, c, w_mod):

    def _A_ref(x, k_cache, v_cache, wq, wo):
        B, Sq, D = x.shape
        H, Dh = (32, 128)
        q = (lambda x, *d: x.permute(*d))((x @ wq).reshape(B, Sq, H, Dh), 0, 2, 1, 3)
        scores = torch.einsum('bhqd,bhkd->bhqk', q, k_cache) * Dh ** (-0.5)
        p = _softmax(scores, axis=-1)
        o = torch.einsum('bhqk,bhkd->bhqd', p, v_cache)
        return (lambda x, *d: x.permute(*d))(o, 0, 2, 1, 3).reshape(B, Sq, H * Dh) @ wo

    def _B_ref(x, w_gate, w_up, w_down):
        return _gelu(x @ w_gate) * (x @ w_up) @ w_down
    x = A_x
    mod = (_silu(c) @ w_mod).reshape(c.shape[0], 6, 1, -1)
    sa, ca, ga, sb, cb, gb = (mod[:, 0].clone(), mod[:, 1].clone(), mod[:, 2].clone(), mod[:, 3].clone(), mod[:, 4].clone(), mod[:, 5].clone())
    h = _layernorm(x, axis=-1) * (1 + ca) + sa
    x = x + ga * _A_ref(h, A_k_cache, A_v_cache, A_wq, A_wo)
    h = _layernorm(x, axis=-1) * (1 + cb) + sb
    x = x + gb * _B_ref(h, B_w_gate, B_w_up, B_w_down)
    return x
```

```cpp
#include <hip/hip_runtime.h>
#include <hip/hip_bf16.h>
#include <stdint.h>

typedef unsigned int u32;
typedef __attribute__((ext_vector_type(4))) u32 u32x4;
typedef __attribute__((ext_vector_type(2))) __bf16 bf16x2_t;

#define NB 4
#define DM 512
#define NH 32
#define SK 4096
#define DH 128
#define FF 1792
#define NMOD 3072

__device__ __forceinline__ float bf_lo(u32 u) { return __uint_as_float(u << 16); }
__device__ __forceinline__ float bf_hi(u32 u) { return __uint_as_float(u & 0xffff0000u); }
__device__ __forceinline__ float bf2f(unsigned short u) { return __uint_as_float(((u32)u) << 16); }
__device__ __forceinline__ float rbf(float x) {
  u32 u = __float_as_uint(x);
  u += 0x7fffu + ((u >> 16) & 1u);
  return __uint_as_float(u & 0xffff0000u);
}
__device__ __forceinline__ u32 pack_bf16(float lo, float hi) {
  return (__float_as_uint(lo) >> 16) | (__float_as_uint(hi) & 0xffff0000u);
}
__device__ __forceinline__ float dot2bf(u32 a, u32 b, float acc) {
  return __builtin_amdgcn_fdot2_f32_bf16(__builtin_bit_cast(bf16x2_t, a), __builtin_bit_cast(bf16x2_t, b), acc, false);
}
template <int CTRL> __device__ __forceinline__ float dppf(float x) {
  return __builtin_bit_cast(float, __builtin_amdgcn_mov_dpp(__builtin_bit_cast(int, x), CTRL, 0xf, 0xf, true));
}
__device__ __forceinline__ float row16_sum(float x) {
  x += dppf<0x128>(x);
  x += dppf<0x124>(x);
  x += dppf<0x122>(x);
  x += dppf<0x121>(x);
  return x;
}
__device__ __forceinline__ float wave_sum(float x) {
#pragma unroll
  for (int m = 32; m >= 1; m >>= 1) x += __shfl_xor(x, m, 64);
  return x;
}
__device__ __forceinline__ u32x4 ld_nt(const void* p) { return __builtin_nontemporal_load(reinterpret_cast<const u32x4*>(p)); }
__device__ __forceinline__ u32x4 ld16(const void* p) { return *reinterpret_cast<const u32x4*>(p); }

template <int N> __device__ __forceinline__ void tr_reduce_step(float (&v)[64], int lane, int mask) {
  const bool up = (lane & mask) != 0;
#pragma unroll
  for (int j = 0; j < N / 2; ++j) {
    const float send = up ? v[j] : v[j + N / 2];
    const float keep = up ? v[j + N / 2] : v[j];
    v[j] = keep + __shfl_xor(send, mask, 64);
  }
}

__device__ __forceinline__ float ln_mod_elem(float xv, float mean_b, float rden, float sc, float sh) {
  const float xm = rbf(xv - mean_b);
  const float nrm = rbf(xm / rden);
  const float t = rbf(nrm * rbf(1.0f + sc));
  return rbf(t + sh);
}

__global__ void __launch_bounds__(512) k_mod(const unsigned short* __restrict__ wmod, const unsigned short* __restrict__ cvec,
                                             float* __restrict__ mod) {
  __shared__ float sc[NB * DM];
  __shared__ float red[8 * 128];
  const int tid = threadIdx.x, lane = tid & 63, wave = tid >> 6;
  const int rr = lane >> 2, cc = lane & 3;
  const int col0 = blockIdx.x * 32 + cc * 8;
  u32x4 w[4];
#pragma unroll
  for (int p = 0; p < 4; ++p) {
    const int row = p * 128 + wave * 16 + rr;
    w[p] = ld_nt(wmod + (size_t)row * NMOD + col0);
  }
  for (int i = tid; i < NB * DM; i += 512) {
    const float v = bf2f(cvec[i]);
    sc[i] = rbf(v / (1.0f + __expf(-v)));
  }
  __syncthreads();
  float acc[64];
#pragma unroll
  for (int j = 0; j < 32; ++j) acc[j] = 0.f;
#pragma unroll
  for (int p = 0; p < 4; ++p) {
    const int row = p * 128 + wave * 16 + rr;
    float wf[8];
#pragma unroll
    for (int k = 0; k < 4; ++k) { wf[2 * k] = bf_lo(w[p][k]); wf[2 * k + 1] = bf_hi(w[p][k]); }
#pragma unroll
    for (int b = 0; b < NB; ++b) {
      const float s = sc[b * DM + row];
#pragma unroll
      for (int j = 0; j < 8; ++j) acc[b * 8 + j] = fmaf(s, wf[j], acc[b * 8 + j]);
    }
  }
  tr_reduce_step<32>(acc, lane, 32);
  tr_reduce_step<16>(acc, lane, 16);
  tr_reduce_step<8>(acc, lane, 8);
  tr_reduce_step<4>(acc, lane, 4);
  {
    const int hi = ((lane >> 5) & 1) * 16 + ((lane >> 4) & 1) * 8 + ((lane >> 3) & 1) * 4 + ((lane >> 2) & 1) * 2;
#pragma unroll
    for (int t = 0; t < 2; ++t) {
      const int j = hi + t;
      const int b = j >> 3, jj = j & 7;
      red[wave * 128 + b * 32 + cc * 8 + jj] = acc[t];
    }
  }
  __syncthreads();
  if (tid < 128) {
    float s = 0.f;
#pragma unroll
    for (int wv = 0; wv < 8; ++wv) s += red[wv * 128 + tid];
    const int b = tid >> 5, col = tid & 31;
    mod[b * NMOD + blockIdx.x * 32 + col] = rbf(s);
  }
}

template <int WAVES, int SPLITS>
__global__ void __launch_bounds__(WAVES * 64) k_attn(const unsigned short* __restrict__ x, const unsigned short* __restrict__ kc,
                                                     const unsigned short* __restrict__ vc, const unsigned short* __restrict__ wq,
                                                     const unsigned short* __restrict__ wo, const float* __restrict__ mod,
                                                     float* __restrict__ pout, float* __restrict__ pml) {
  constexpr int NT = WAVES * 64;
  constexpr int KEYS_WG = SK / SPLITS;
  constexpr int TILES = KEYS_WG / (16 * WAVES);
  constexpr int NG = WAVES * 4;
  __shared__ float hvec[DM];
  __shared__ float stat[2];
  __shared__ float qred[WAVES * 4 * DH];
  __shared__ float gml[NG * 2];
  __shared__ float ofin[DH];

  const int tid = threadIdx.x, lane = tid & 63, wave = tid >> 6;
  const int r = lane >> 4, c = lane & 15;
  const int split = blockIdx.x % SPLITS;
  const int bh = blockIdx.x / SPLITS;
  const int b = bh / NH, h = bh % NH;

  const unsigned short* kbase = kc + ((size_t)bh * SK + (size_t)split * KEYS_WG) * DH + (size_t)r * DH + c * 8;
  const unsigned short* vbase = vc + ((size_t)bh * SK + (size_t)split * KEYS_WG) * DH + (size_t)r * DH + c * 8;

  u32x4 kA[4], vA[4], kB[4], vB[4];
  {
    const size_t off = (size_t)wave * 16 * DH;
#pragma unroll
    for (int i = 0; i < 4; ++i) kA[i] = ld_nt(kbase + off + (size_t)i * 4 * DH);
#pragma unroll
    for (int i = 0; i < 4; ++i) vA[i] = ld_nt(vbase + off + (size_t)i * 4 * DH);
  }

  constexpr int QP = DM / (4 * WAVES);
  u32x4 wqv[QP];
#pragma unroll
  for (int p = 0; p < QP; ++p) {
    const int row = p * (4 * WAVES) + wave * 4 + r;
    wqv[p] = ld16(wq + (size_t)row * (NH * DH) + h * DH + c * 8);
  }

  {
    float s = 0.f, s2 = 0.f;
    if (wave == 0) {
      float xv[8];
      const u32x4 xx = ld16(x + b * DM + lane * 8);
#pragma unroll
      for (int k = 0; k < 4; ++k) { xv[2 * k] = bf_lo(xx[k]); xv[2 * k + 1] = bf_hi(xx[k]); }
#pragma unroll
      for (int k = 0; k < 8; ++k) s += xv[k];
      s = wave_sum(s);
      const float mean = s * (1.0f / DM);
#pragma unroll
      for (int k = 0; k < 8; ++k) { const float d = xv[k] - mean; s2 += d * d; }
      s2 = wave_sum(s2);
      const float var = s2 * (1.0f / DM);
      const float mean_b = rbf(mean);
      const float rden = rbf(sqrtf(rbf(rbf(var) + 1e-5f)));
#pragma unroll
      for (int k = 0; k < 8; ++k) {
        const int n = lane * 8 + k;
        hvec[n] = ln_mod_elem(xv[k], mean_b, rden, mod[b * NMOD + DM + n], mod[b * NMOD + n]);
      }
    }
  }
  __syncthreads();

  {
    float qa[8];
#pragma unroll
    for (int j = 0; j < 8; ++j) qa[j] = 0.f;
#pragma unroll
    for (int p = 0; p < QP; ++p) {
      const int row = p * (4 * WAVES) + wave * 4 + r;
      const float hv = hvec[row];
#pragma unroll
      for (int k = 0; k < 4; ++k) {
        qa[2 * k] = fmaf(hv, bf_lo(wqv[p][k]), qa[2 * k]);
        qa[2 * k + 1] = fmaf(hv, bf_hi(wqv[p][k]), qa[2 * k + 1]);
      }
    }
#pragma unroll
    for (int j = 0; j < 8; ++j) qred[(wave * 4 + r) * DH + c * 8 + j] = qa[j];
  }
  __syncthreads();
  u32 qpk[4];
  {
    float qs[8];
#pragma unroll
    for (int j = 0; j < 8; ++j) qs[j] = 0.f;
    for (int g = 0; g < WAVES * 4; ++g) {
#pragma unroll
      for (int j = 0; j < 8; ++j) qs[j] += qred[g * DH + c * 8 + j];
    }
#pragma unroll
    for (int k = 0; k < 4; ++k) qpk[k] = pack_bf16(rbf(qs[2 * k]), rbf(qs[2 * k + 1]));
  }

  const float SCALE = 0.08838834764831845f;
  const float LOG2E = 1.4426950408889634f;
  float m_run = -INFINITY, l_run = 0.f;
  float o[8];
#pragma unroll
  for (int j = 0; j < 8; ++j) o[j] = 0.f;

  auto compute = [&](const u32x4 (&kt)[4], const u32x4 (&vt)[4]) {
    float sc[4];
#pragma unroll
    for (int i = 0; i < 4; ++i) {
      float d = 0.f;
#pragma unroll
      for (int k = 0; k < 4; ++k) d = dot2bf(kt[i][k], qpk[k], d);
      d = row16_sum(d);
      sc[i] = rbf(rbf(d) * SCALE) * LOG2E;
    }
    const float mt = fmaxf(fmaxf(sc[0], sc[1]), fmaxf(sc[2], sc[3]));
    const float m_new = fmaxf(m_run, mt);
    const float alpha = __builtin_amdgcn_exp2f(m_run - m_new);
    float p[4];
#pragma unroll
    for (int i = 0; i < 4; ++i) p[i] = __builtin_amdgcn_exp2f(sc[i] - m_new);
    l_run = fmaf(l_run, alpha, (p[0] + p[1]) + (p[2] + p[3]));
    m_run = m_new;
#pragma unroll
    for (int j = 0; j < 8; ++j) o[j] *= alpha;
#pragma unroll
    for (int i = 0; i < 4; ++i) {
#pragma unroll
      for (int k = 0; k < 4; ++k) {
        o[2 * k] = fmaf(p[i], bf_lo(vt[i][k]), o[2 * k]);
        o[2 * k + 1] = fmaf(p[i], bf_hi(vt[i][k]), o[2 * k + 1]);
      }
    }
  };
  auto load_tile = [&](int t, u32x4 (&kt)[4], u32x4 (&vt)[4]) {
    const size_t off = (size_t)(t * WAVES + wave) * 16 * DH;
#pragma unroll
    for (int i = 0; i < 4; ++i) kt[i] = ld_nt(kbase + off + (size_t)i * 4 * DH);
#pragma unroll
    for (int i = 0; i < 4; ++i) vt[i] = ld_nt(vbase + off + (size_t)i * 4 * DH);
  };

  static_assert(TILES % 2 == 0, "tiles per wave must be even");
  for (int t = 0; t < TILES; t += 2) {
    load_tile(t + 1, kB, vB);
    compute(kA, vA);
    if (t + 2 < TILES) load_tile(t + 2, kA, vA);
    compute(kB, vB);
  }

  __syncthreads();
  {
    const int g = wave * 4 + r;
#pragma unroll
    for (int j = 0; j < 8; ++j) qred[g * DH + c * 8 + j] = o[j];
    if (c == 0) { gml[g * 2] = m_run; gml[g * 2 + 1] = l_run; }
  }
  __syncthreads();
  if (tid < DH) {
    float M = -INFINITY;
    for (int g = 0; g < NG; ++g) M = fmaxf(M, gml[g * 2]);
    float L = 0.f, od = 0.f;
    for (int g = 0; g < NG; ++g) {
      const float wt = __builtin_amdgcn_exp2f(gml[g * 2] - M);
      L = fmaf(wt, gml[g * 2 + 1], L);
      od = fmaf(wt, qred[g * DH + tid], od);
    }
    ofin[tid] = od;
    if (tid == 0) {
      pml[(size_t)blockIdx.x * 2] = M;
      pml[(size_t)blockIdx.x * 2 + 1] = L;
    }
  }
  __syncthreads();

  {
    constexpr int RW = DH / WAVES;
    float pa[8];
#pragma unroll
    for (int j = 0; j < 8; ++j) pa[j] = 0.f;
    u32x4 wv[RW];
#pragma unroll
    for (int i = 0; i < RW; ++i) {
      const int d = wave * RW + i;
      wv[i] = ld16(wo + (size_t)(h * DH + d) * DM + lane * 8);
    }
#pragma unroll
    for (int i = 0; i < RW; ++i) {
      const float ov = ofin[wave * RW + i];
#pragma unroll
      for (int k = 0; k < 4; ++k) {
        pa[2 * k] = fmaf(ov, bf_lo(wv[i][k]), pa[2 * k]);
        pa[2 * k + 1] = fmaf(ov, bf_hi(wv[i][k]), pa[2 * k + 1]);
      }
    }
#pragma unroll
    for (int j = 0; j < 8; ++j) qred[wave * DM + lane * 8 + j] = pa[j];
    __syncthreads();
    for (int n = tid; n < DM; n += NT) {
      float s = 0.f;
#pragma unroll
      for (int wv2 = 0; wv2 < WAVES; ++wv2) s += qred[wv2 * DM + n];
      pout[(size_t)blockIdx.x * DM + n] = s;
    }
  }
}

template <int SPLITS>
__global__ void __launch_bounds__(256) k_red1(const unsigned short* __restrict__ x, const float* __restrict__ mod,
                                              const float* __restrict__ pout, const float* __restrict__ pml, float* __restrict__ x1) {
  __shared__ float red[4 * 64];
  const int b = blockIdx.x >> 3, cb = blockIdx.x & 7;
  const int tid = threadIdx.x, col = tid & 63, g = tid >> 6;
  const int n = cb * 64 + col;
  float acc = 0.f;
  for (int h = g; h < NH; h += 4) {
    const int base = (b * NH + h) * SPLITS;
    float M = -INFINITY;
#pragma unroll
    for (int s = 0; s < SPLITS; ++s) M = fmaxf(M, pml[(base + s) * 2]);
    float L = 0.f, a = 0.f;
#pragma unroll
    for (int s = 0; s < SPLITS; ++s) {
      const float wt = __builtin_amdgcn_exp2f(pml[(base + s) * 2] - M);
      L = fmaf(wt, pml[(base + s) * 2 + 1], L);
      a = fmaf(wt, pout[(size_t)(base + s) * DM + n], a);
    }
    acc += a / L;
  }
  red[g * 64 + col] = acc;
  __syncthreads();
  if (tid < 64) {
    const float attn = rbf(red[col] + red[64 + col] + red[128 + col] + red[192 + col]);
    const float ga = mod[b * NMOD + 2 * DM + n];
    x1[b * DM + n] = rbf(bf2f(x[b * DM + n]) + rbf(ga * attn));
  }
}

__global__ void __launch_bounds__(512) k_ffn(const float* __restrict__ x1, const float* __restrict__ mod,
                                             const unsigned short* __restrict__ wg, const unsigned short* __restrict__ wu,
                                             const unsigned short* __restrict__ wd, float* __restrict__ p2) {
  __shared__ float h2[NB * DM];
  __shared__ float red[8 * 128];
  __shared__ float act[NB * 16];
  __shared__ float red2[8 * DM];
  const int tid = threadIdx.x, lane = tid & 63, wave = tid >> 6;
  const int wgid = (blockIdx.x & 7) * 14 + (blockIdx.x >> 3);
  const int f0 = wgid * 16;
  const int rr = lane >> 1, cc = lane & 1;
  u32x4 g4[2], u4[2];
#pragma unroll
  for (int p = 0; p < 2; ++p) {
    const int row = p * 256 + wave * 32 + rr;
    g4[p] = ld_nt(wg + (size_t)row * FF + f0 + cc * 8);
    u4[p] = ld_nt(wu + (size_t)row * FF + f0 + cc * 8);
  }
  u32x4 d4[2];
#pragma unroll
  for (int i = 0; i < 2; ++i) d4[i] = ld_nt(wd + (size_t)(f0 + wave * 2 + i) * DM + lane * 8);

  if (wave < NB) {
    const int b = wave;
    float xv[8];
#pragma unroll
    for (int k = 0; k < 8; ++k) xv[k] = x1[b * DM + lane * 8 + k];
    float s = 0.f;
#pragma unroll
    for (int k = 0; k < 8; ++k) s += xv[k];
    s = wave_sum(s);
    const float mean = s * (1.0f / DM);
    float s2 = 0.f;
#pragma unroll
    for (int k = 0; k < 8; ++k) { const float d = xv[k] - mean; s2 += d * d; }
    s2 = wave_sum(s2);
    const float var = s2 * (1.0f / DM);
    const float mean_b = rbf(mean);
    const float rden = rbf(sqrtf(rbf(rbf(var) + 1e-5f)));
#pragma unroll
    for (int k = 0; k < 8; ++k) {
      const int n = lane * 8 + k;
      h2[b * DM + n] = ln_mod_elem(xv[k], mean_b, rden, mod[b * NMOD + 4 * DM + n], mod[b * NMOD + 3 * DM + n]);
    }
  }
  __syncthreads();
  float acc[64];
#pragma unroll
  for (int j = 0; j < 64; ++j) acc[j] = 0.f;
#pragma unroll
  for (int p = 0; p < 2; ++p) {
    const int row = p * 256 + wave * 32 + rr;
    float gf[8], uf[8];
#pragma unroll
    for (int k = 0; k < 4; ++k) {
      gf[2 * k] = bf_lo(g4[p][k]); gf[2 * k + 1] = bf_hi(g4[p][k]);
      uf[2 * k] = bf_lo(u4[p][k]); uf[2 * k + 1] = bf_hi(u4[p][k]);
    }
#pragma unroll
    for (int b = 0; b < NB; ++b) {
      const float hv = h2[b * DM + row];
#pragma unroll
      for (int j = 0; j < 8; ++j) {
        acc[b * 8 + j] = fmaf(hv, gf[j], acc[b * 8 + j]);
        acc[32 + b * 8 + j] = fmaf(hv, uf[j], acc[32 + b * 8 + j]);
      }
    }
  }
  tr_reduce_step<64>(acc, lane, 32);
  tr_reduce_step<32>(acc, lane, 16);
  tr_reduce_step<16>(acc, lane, 8);
  tr_reduce_step<8>(acc, lane, 4);
  tr_reduce_step<4>(acc, lane, 2);
  {
    const int hi = ((lane >> 5) & 1) * 32 + ((lane >> 4) & 1) * 16 + ((lane >> 3) & 1) * 8 + ((lane >> 2) & 1) * 4 + ((lane >> 1) & 1) * 2;
#pragma unroll
    for (int t = 0; t < 2; ++t) {
      const int j = hi + t;
      const int mat = j >> 5, b = (j >> 3) & 3, jj = j & 7;
      red[wave * 128 + mat * 64 + b * 16 + cc * 8 + jj] = acc[t];
    }
  }
  __syncthreads();
  if (tid < 64) {
    float g = 0.f, u = 0.f;
#pragma unroll
    for (int wv = 0; wv < 8; ++wv) { g += red[wv * 128 + tid]; u += red[wv * 128 + 64 + tid]; }
    g = rbf(g); u = rbf(u);
    const float inner = 0.7978845608028654f * (g + 0.044715f * g * g * g);
    const float ge = rbf(0.5f * g * (1.0f + tanhf(inner)));
    act[tid] = rbf(ge * u);
  }
  __syncthreads();
  {
#pragma unroll
    for (int b = 0; b < NB; ++b) {
      float pa[8];
#pragma unroll
      for (int j = 0; j < 8; ++j) pa[j] = 0.f;
#pragma unroll
      for (int i = 0; i < 2; ++i) {
        const float av = act[b * 16 + wave * 2 + i];
#pragma unroll
        for (int k = 0; k < 4; ++k) {
          pa[2 * k] = fmaf(av, bf_lo(d4[i][k]), pa[2 * k]);
          pa[2 * k + 1] = fmaf(av, bf_hi(d4[i][k]), pa[2 * k + 1]);
        }
      }
#pragma unroll
      for (int j = 0; j < 8; ++j) red2[wave * DM + lane * 8 + j] = pa[j];
      __syncthreads();
      {
        const int n = tid;
        float s = 0.f;
#pragma unroll
        for (int wv = 0; wv < 8; ++wv) s += red2[wv * DM + n];
        p2[((size_t)wgid * NB + b) * DM + n] = s;
      }
      __syncthreads();
    }
  }
}

__global__ void __launch_bounds__(256) k_red2(const float* __restrict__ x1, const float* __restrict__ mod, const float* __restrict__ p2,
                                              __hip_bfloat16* __restrict__ out) {
  __shared__ float red[4 * 64];
  const int b = blockIdx.x >> 3, cb = blockIdx.x & 7;
  const int tid = threadIdx.x, col = tid & 63, g = tid >> 6;
  const int n = cb * 64 + col;
  float acc = 0.f;
  for (int w = g; w < 112; w += 4) acc += p2[((size_t)w * NB + b) * DM + n];
  red[g * 64 + col] = acc;
  __syncthreads();
  if (tid < 64) {
    const float y = rbf(red[col] + red[64 + col] + red[128 + col] + red[192 + col]);
    const float gb = mod[b * NMOD + 5 * DM + n];
    out[b * DM + n] = __float2bfloat16(x1[b * DM + n] + rbf(gb * y));
  }
}

extern "C" void kernel_launch(void* const* d_in, const int* in_sizes, int n_in, void* d_out, int out_size, void* d_ws, size_t ws_size,
                              hipStream_t stream) {
  const unsigned short* x = (const unsigned short*)d_in[0];
  const unsigned short* kc = (const unsigned short*)d_in[1];
  const unsigned short* vc = (const unsigned short*)d_in[2];
  const unsigned short* wq = (const unsigned short*)d_in[3];
  const unsigned short* wo = (const unsigned short*)d_in[4];
  const unsigned short* wg = (const unsigned short*)d_in[6];
  const unsigned short* wu = (const unsigned short*)d_in[7];
  const unsigned short* wd = (const unsigned short*)d_in[8];
  const unsigned short* cv = (const unsigned short*)d_in[9];
  const unsigned short* wmod = (const unsigned short*)d_in[10];

  constexpr int WAVES = 8, SPLITS = 2;
  float* ws = (float*)d_ws;
  float* mod = ws;
  float* x1 = mod + NB * NMOD;
  float* pml = x1 + NB * DM;
  float* pout = pml + NB * NH * SPLITS * 2;
  float* p2 = pout + (size_t)NB * NH * SPLITS * DM;

  k_mod<<<96, 512, 0, stream>>>(wmod, cv, mod);
  k_attn<WAVES, SPLITS><<<NB * NH * SPLITS, WAVES * 64, 0, stream>>>(x, kc, vc, wq, wo, mod, pout, pml);
  k_red1<SPLITS><<<NB * 8, 256, 0, stream>>>(x, mod, pout, pml, x1);
  k_ffn<<<112, 512, 0, stream>>>(x1, mod, wg, wu, wd, p2);
  k_red2<<<NB * 8, 256, 0, stream>>>(x1, mod, p2, (__hip_bfloat16*)d_out);
}
```

```cpp
#include <hip/hip_runtime.h>
#include <hip/hip_bf16.h>
#include <stdint.h>
#include <type_traits>

typedef unsigned int u32;
typedef __attribute__((ext_vector_type(4))) u32 u32x4;
typedef __attribute__((ext_vector_type(2))) __bf16 bf16x2_t;

#define NB 4
#define DM 512
#define NH 32
#define SK 4096
#define DH 128
#define FF 1792
#define NMOD 3072

__device__ __forceinline__ float bf_lo(u32 u) { return __uint_as_float(u << 16); }
__device__ __forceinline__ float bf_hi(u32 u) { return __uint_as_float(u & 0xffff0000u); }
__device__ __forceinline__ float bf2f(unsigned short u) { return __uint_as_float(((u32)u) << 16); }
__device__ __forceinline__ float rbf(float x) {
  u32 u = __float_as_uint(x);
  u += 0x7fffu + ((u >> 16) & 1u);
  return __uint_as_float(u & 0xffff0000u);
}
__device__ __forceinline__ u32 pack_bf16(float lo, float hi) {
  return (__float_as_uint(lo) >> 16) | (__float_as_uint(hi) & 0xffff0000u);
}
__device__ __forceinline__ float dot2bf(u32 a, u32 b, float acc) {
  return __builtin_amdgcn_fdot2_f32_bf16(__builtin_bit_cast(bf16x2_t, a), __builtin_bit_cast(bf16x2_t, b), acc, false);
}
template <int CTRL> __device__ __forceinline__ float dppf(float x) {
  return __builtin_bit_cast(float, __builtin_amdgcn_mov_dpp(__builtin_bit_cast(int, x), CTRL, 0xf, 0xf, true));
}
__device__ __forceinline__ float row16_sum(float x) {
  x += dppf<0x128>(x);
  x += dppf<0x124>(x);
  x += dppf<0x122>(x);
  x += dppf<0x121>(x);
  return x;
}
__device__ __forceinline__ float wave_sum(float x) {
  x = row16_sum(x);
  x += __shfl_xor(x, 16, 64);
  x += __shfl_xor(x, 32, 64);
  return x;
}
__device__ __forceinline__ u32x4 ld_nt(const void* p) { return __builtin_nontemporal_load(reinterpret_cast<const u32x4*>(p)); }
__device__ __forceinline__ u32x4 ld16(const void* p) { return *reinterpret_cast<const u32x4*>(p); }

typedef unsigned long long u64;
__device__ __forceinline__ void st_granule(u64* p, float v) {
  __hip_atomic_store(p, (1ull << 32) | (u64)__float_as_uint(v), __ATOMIC_RELAXED, __HIP_MEMORY_SCOPE_AGENT);
}
__device__ __forceinline__ u64 ld_granule(const u64* p) { return __hip_atomic_load(p, __ATOMIC_RELAXED, __HIP_MEMORY_SCOPE_AGENT); }

template <int N> __device__ __forceinline__ void tr_reduce_step(float (&v)[64], int lane, int mask) {
  const bool up = (lane & mask) != 0;
#pragma unroll
  for (int j = 0; j < N / 2; ++j) {
    const float send = up ? v[j] : v[j + N / 2];
    const float keep = up ? v[j + N / 2] : v[j];
    v[j] = keep + __shfl_xor(send, mask, 64);
  }
}

__device__ __forceinline__ float ln_mod_elem(float xv, float mean_b, float rden, float sc, float sh) {
  const float xm = rbf(xv - mean_b);
  const float nrm = rbf(xm / rden);
  const float t = rbf(nrm * rbf(1.0f + sc));
  return rbf(t + sh);
}

__global__ void __launch_bounds__(512) k_mod(const unsigned short* __restrict__ wmod, const unsigned short* __restrict__ cvec,
                                             float* __restrict__ mod, u32x4* __restrict__ gran, int gran16) {
  __shared__ float sc[NB * DM];
  __shared__ float red[8 * 128];
  const int tid = threadIdx.x, lane = tid & 63, wave = tid >> 6;
  const int rr = lane >> 2, cc = lane & 3;
  const int col0 = blockIdx.x * 32 + cc * 8;
  float cvv[4];
#pragma unroll
  for (int i = 0; i < 4; ++i) cvv[i] = bf2f(cvec[tid + i * 512]);
  u32x4 w[4];
#pragma unroll
  for (int p = 0; p < 4; ++p) {
    const int row = p * 128 + wave * 16 + rr;
    w[p] = ld_nt(wmod + (size_t)row * NMOD + col0);
  }
#pragma unroll
  for (int i = 0; i < 4; ++i) sc[tid + i * 512] = rbf(cvv[i] / (1.0f + __expf(-cvv[i])));
  for (int i = blockIdx.x * 512 + tid; i < gran16; i += gridDim.x * 512) gran[i] = u32x4{0u, 0u, 0u, 0u};
  __syncthreads();
  float acc[64];
#pragma unroll
  for (int j = 0; j < 32; ++j) acc[j] = 0.f;
#pragma unroll
  for (int p = 0; p < 4; ++p) {
    const int row = p * 128 + wave * 16 + rr;
    float wf[8];
#pragma unroll
    for (int k = 0; k < 4; ++k) { wf[2 * k] = bf_lo(w[p][k]); wf[2 * k + 1] = bf_hi(w[p][k]); }
#pragma unroll
    for (int b = 0; b < NB; ++b) {
      const float s = sc[b * DM + row];
#pragma unroll
      for (int j = 0; j < 8; ++j) acc[b * 8 + j] = fmaf(s, wf[j], acc[b * 8 + j]);
    }
  }
  tr_reduce_step<32>(acc, lane, 32);
  tr_reduce_step<16>(acc, lane, 16);
  tr_reduce_step<8>(acc, lane, 8);
  tr_reduce_step<4>(acc, lane, 4);
  {
    const int hi = ((lane >> 5) & 1) * 16 + ((lane >> 4) & 1) * 8 + ((lane >> 3) & 1) * 4 + ((lane >> 2) & 1) * 2;
#pragma unroll
    for (int t = 0; t < 2; ++t) {
      const int j = hi + t;
      const int b = j >> 3, jj = j & 7;
      red[wave * 128 + b * 32 + cc * 8 + jj] = acc[t];
    }
  }
  __syncthreads();
  if (tid < 128) {
    float s = 0.f;
#pragma unroll
    for (int wv = 0; wv < 8; ++wv) s += red[wv * 128 + tid];
    const int b = tid >> 5, col = tid & 31;
    mod[b * NMOD + blockIdx.x * 32 + col] = rbf(s);
  }
}

#define GRP 8
#define OREC 132
template <int WAVES>
__global__ void __launch_bounds__(WAVES * 64) k_attn(const unsigned short* __restrict__ x, const unsigned short* __restrict__ kc,
                                                     const unsigned short* __restrict__ vc, const unsigned short* __restrict__ wq,
                                                     const unsigned short* __restrict__ wo, const float* __restrict__ mod,
                                                     float* __restrict__ pout, u64* __restrict__ qg, u64* __restrict__ og) {
  constexpr int SPLITS = 2;
  constexpr int NT = WAVES * 64;
  constexpr int KEYS_WG = SK / SPLITS;
  constexpr int TILES = KEYS_WG / (16 * WAVES);
  constexpr int NG = WAVES * 4;
  __shared__ float hvec[NB * DM];
  __shared__ float qred[NG * NB * DH];
  __shared__ float gml[NG * 2];
  __shared__ float ofin[NB * DH];

  const int tid = threadIdx.x, lane = tid & 63, wave = tid >> 6;
  const int r = lane >> 4, c = lane & 15;
  const int h = blockIdx.x / GRP, member = blockIdx.x % GRP;
  const int b = member >> 1, split = member & 1;
  const int bh = b * NH + h;
  const bool leader = (member == 0);

  const unsigned short* kbase = kc + ((size_t)bh * SK + (size_t)split * KEYS_WG) * DH + (size_t)r * DH + c * 8;
  const unsigned short* vbase = vc + ((size_t)bh * SK + (size_t)split * KEYS_WG) * DH + (size_t)r * DH + c * 8;

  auto produce_q = [&](auto chunk_tag) {
    constexpr int QP = DM / (4 * WAVES);
    constexpr int CH = decltype(chunk_tag)::value;
    float xv[8], mca[8], msa[8];
    if (wave < NB) {
      const u32x4 xx = ld16(x + wave * DM + lane * 8);
#pragma unroll
      for (int k = 0; k < 4; ++k) { xv[2 * k] = bf_lo(xx[k]); xv[2 * k + 1] = bf_hi(xx[k]); }
#pragma unroll
      for (int k = 0; k < 8; ++k) { mca[k] = mod[wave * NMOD + DM + lane * 8 + k]; msa[k] = mod[wave * NMOD + lane * 8 + k]; }
    }
    u32x4 wqv[CH];
#pragma unroll
    for (int p = 0; p < CH; ++p) {
      const int row = p * (4 * WAVES) + wave * 4 + r;
      wqv[p] = ld_nt(wq + (size_t)row * (NH * DH) + h * DH + c * 8);
    }
    if (wave < NB) {
      float s = 0.f, s2 = 0.f;
#pragma unroll
      for (int k = 0; k < 8; ++k) s += xv[k];
      s = wave_sum(s);
      const float mean = s * (1.0f / DM);
#pragma unroll
      for (int k = 0; k < 8; ++k) { const float d = xv[k] - mean; s2 += d * d; }
      s2 = wave_sum(s2);
      const float var = s2 * (1.0f / DM);
      const float mean_b = rbf(mean);
      const float rden = rbf(sqrtf(rbf(rbf(var) + 1e-5f)));
#pragma unroll
      for (int k = 0; k < 8; ++k) hvec[wave * DM + lane * 8 + k] = ln_mod_elem(xv[k], mean_b, rden, mca[k], msa[k]);
    }
    __syncthreads();
    float qa[NB][8];
#pragma unroll
    for (int bb = 0; bb < NB; ++bb)
#pragma unroll
      for (int j = 0; j < 8; ++j) qa[bb][j] = 0.f;
#pragma unroll 1
    for (int p0 = 0; p0 < QP; p0 += CH) {
      if (p0 > 0) {
#pragma unroll
        for (int p = 0; p < CH; ++p) {
          const int row = (p0 + p) * (4 * WAVES) + wave * 4 + r;
          wqv[p] = ld_nt(wq + (size_t)row * (NH * DH) + h * DH + c * 8);
        }
      }
#pragma unroll
      for (int p = 0; p < CH; ++p) {
        const int row = (p0 + p) * (4 * WAVES) + wave * 4 + r;
#pragma unroll
        for (int bb = 0; bb < NB; ++bb) {
          const float hv = hvec[bb * DM + row];
#pragma unroll
          for (int k = 0; k < 4; ++k) {
            qa[bb][2 * k] = fmaf(hv, bf_lo(wqv[p][k]), qa[bb][2 * k]);
            qa[bb][2 * k + 1] = fmaf(hv, bf_hi(wqv[p][k]), qa[bb][2 * k + 1]);
          }
        }
      }
    }
#pragma unroll
    for (int bb = 0; bb < NB; ++bb)
#pragma unroll
      for (int j = 0; j < 8; ++j) qred[((wave * 4 + r) * NB + bb) * DH + c * 8 + j] = qa[bb][j];
    __syncthreads();
    for (int i = tid; i < NB * DH; i += NT) {
      float s = 0.f;
      for (int g = 0; g < NG; ++g) s += qred[g * NB * DH + i];
      st_granule(qg + (size_t)h * NB * DH + i, rbf(s));
    }
    __syncthreads();
  };

  if (leader) produce_q(std::integral_constant<int, DM / (4 * WAVES)>{});

  u32x4 kA[4], vA[4], kB[4], vB[4];
  {
    const size_t off = (size_t)wave * 16 * DH;
#pragma unroll
    for (int i = 0; i < 4; ++i) kA[i] = ld_nt(kbase + off + (size_t)i * 4 * DH);
#pragma unroll
    for (int i = 0; i < 4; ++i) vA[i] = ld_nt(vbase + off + (size_t)i * 4 * DH);
    const size_t off2 = (size_t)(WAVES + wave) * 16 * DH;
#pragma unroll
    for (int i = 0; i < 4; ++i) kB[i] = ld_nt(kbase + off2 + (size_t)i * 4 * DH);
#pragma unroll
    for (int i = 0; i < 4; ++i) vB[i] = ld_nt(vbase + off2 + (size_t)i * 4 * DH);
  }

  u32 qpk[4];
  {
    const u64* qp = qg + ((size_t)h * NB + b) * DH + c * 8;
    float qv[8];
    for (int attempt = 0;; ++attempt) {
      bool ok = true;
      for (int spin = 0; spin < 20000; ++spin) {
        ok = true;
#pragma unroll
        for (int j = 0; j < 8; ++j) { const u64 g = ld_granule(qp + j); qv[j] = __uint_as_float((u32)g); ok = ok && ((g >> 32) == 1ull); }
        if (__syncthreads_and(ok)) { ok = true; break; }
        ok = false;
        __builtin_amdgcn_s_sleep(2);
      }
      if (ok) break;
      produce_q(std::integral_constant<int, 1>{});
    }
#pragma unroll
    for (int k = 0; k < 4; ++k) qpk[k] = pack_bf16(qv[2 * k], qv[2 * k + 1]);
  }

  const float SCALE = 0.08838834764831845f;
  const float LOG2E = 1.4426950408889634f;
  float m_run = -INFINITY, l_run = 0.f;
  float o[8];
#pragma unroll
  for (int j = 0; j < 8; ++j) o[j] = 0.f;

  auto compute = [&](const u32x4 (&kt)[4], const u32x4 (&vt)[4]) {
    float sc[4];
#pragma unroll
    for (int i = 0; i < 4; ++i) {
      float d = 0.f;
#pragma unroll
      for (int k = 0; k < 4; ++k) d = dot2bf(kt[i][k], qpk[k], d);
      d = row16_sum(d);
      sc[i] = rbf(rbf(d) * SCALE) * LOG2E;
    }
    const float mt = fmaxf(fmaxf(sc[0], sc[1]), fmaxf(sc[2], sc[3]));
    const float m_new = fmaxf(m_run, mt);
    const float alpha = __builtin_amdgcn_exp2f(m_run - m_new);
    float p[4];
#pragma unroll
    for (int i = 0; i < 4; ++i) p[i] = __builtin_amdgcn_exp2f(sc[i] - m_new);
    l_run = fmaf(l_run, alpha, (p[0] + p[1]) + (p[2] + p[3]));
    m_run = m_new;
#pragma unroll
    for (int j = 0; j < 8; ++j) o[j] *= alpha;
#pragma unroll
    for (int i = 0; i < 4; ++i) {
#pragma unroll
      for (int k = 0; k < 4; ++k) {
        o[2 * k] = fmaf(p[i], bf_lo(vt[i][k]), o[2 * k]);
        o[2 * k + 1] = fmaf(p[i], bf_hi(vt[i][k]), o[2 * k + 1]);
      }
    }
  };
  auto load_tile = [&](int t, u32x4 (&kt)[4], u32x4 (&vt)[4]) {
    const size_t off = (size_t)(t * WAVES + wave) * 16 * DH;
#pragma unroll
    for (int i = 0; i < 4; ++i) kt[i] = ld_nt(kbase + off + (size_t)i * 4 * DH);
#pragma unroll
    for (int i = 0; i < 4; ++i) vt[i] = ld_nt(vbase + off + (size_t)i * 4 * DH);
  };

  static_assert(TILES % 2 == 0 && TILES >= 4, "tiles per wave must be even");
  for (int t = 0; t < TILES - 2; t += 2) {
    compute(kA, vA);
    load_tile(t + 2, kA, vA);
    compute(kB, vB);
    load_tile(t + 3, kB, vB);
  }
  constexpr int RW = DH / WAVES;
  u32x4 wv[RW];
  if (leader) {
#pragma unroll
    for (int i = 0; i < RW; ++i) wv[i] = ld_nt(wo + (size_t)(h * DH + wave * RW + i) * DM + lane * 8);
  }
  compute(kA, vA);
  compute(kB, vB);

  __syncthreads();
  {
    const int g = wave * 4 + r;
#pragma unroll
    for (int j = 0; j < 8; ++j) qred[g * DH + c * 8 + j] = o[j];
    if (c == 0) { gml[g * 2] = m_run; gml[g * 2 + 1] = l_run; }
  }
  __syncthreads();
  float myM = 0.f, myL = 0.f, myO = 0.f;
  if (tid < DH) {
    float M = -INFINITY;
    for (int g = 0; g < NG; ++g) M = fmaxf(M, gml[g * 2]);
    float L = 0.f, od = 0.f;
    for (int g = 0; g < NG; ++g) {
      const float wt = __builtin_amdgcn_exp2f(gml[g * 2] - M);
      L = fmaf(wt, gml[g * 2 + 1], L);
      od = fmaf(wt, qred[g * DH + tid], od);
    }
    myM = M; myL = L; myO = od;
    u64* rec = og + ((size_t)h * GRP + member) * OREC;
    st_granule(rec + 4 + tid, od);
    if (tid == 0) { st_granule(rec, M); st_granule(rec + 1, L); }
  }
  if (!leader) return;

  {
    for (int i = tid; i < NB * DH; i += NT) {
      const int bb = i / DH, d = i % DH;
      const u64* r0 = og + ((size_t)h * GRP + bb * 2) * OREC;
      const u64* r1 = r0 + OREC;
      float m0, l0, o0, m1, l1, o1;
      for (int spin = 0; spin < (1 << 24); ++spin) {
        const u64 a0 = ld_granule(r0), a1 = ld_granule(r0 + 1), a2 = ld_granule(r0 + 4 + d);
        const u64 b0 = ld_granule(r1), b1 = ld_granule(r1 + 1), b2 = ld_granule(r1 + 4 + d);
        m0 = __uint_as_float((u32)a0); l0 = __uint_as_float((u32)a1); o0 = __uint_as_float((u32)a2);
        m1 = __uint_as_float((u32)b0); l1 = __uint_as_float((u32)b1); o1 = __uint_as_float((u32)b2);
        if (((a0 & a1 & a2 & b0 & b1 & b2) >> 32) == 1ull) break;
        __builtin_amdgcn_s_sleep(1);
      }
      const float M = fmaxf(m0, m1);
      const float w0 = __builtin_amdgcn_exp2f(m0 - M), w1 = __builtin_amdgcn_exp2f(m1 - M);
      const float L = fmaf(w0, l0, w1 * l1);
      ofin[i] = fmaf(w0, o0, w1 * o1) / L;
    }
    __syncthreads();
#pragma unroll
    for (int bb = 0; bb < NB; ++bb) {
      float pa[8];
#pragma unroll
      for (int j = 0; j < 8; ++j) pa[j] = 0.f;
#pragma unroll
      for (int i = 0; i < RW; ++i) {
        const float ov = ofin[bb * DH + wave * RW + i];
#pragma unroll
        for (int k = 0; k < 4; ++k) {
          pa[2 * k] = fmaf(ov, bf_lo(wv[i][k]), pa[2 * k]);
          pa[2 * k + 1] = fmaf(ov, bf_hi(wv[i][k]), pa[2 * k + 1]);
        }
      }
#pragma unroll
      for (int j = 0; j < 8; ++j) qred[(wave * NB + bb) * DM + lane * 8 + j] = pa[j];
    }
    __syncthreads();
    for (int i = tid; i < NB * DM; i += NT) {
      const int bb = i / DM, n = i % DM;
      float s = 0.f;
#pragma unroll
      for (int w2 = 0; w2 < WAVES; ++w2) s += qred[(w2 * NB + bb) * DM + n];
      pout[((size_t)h * NB + bb) * DM + n] = s;
    }
  }
}

__global__ void __launch_bounds__(256) k_red1(const unsigned short* __restrict__ x, const float* __restrict__ mod,
                                              const float* __restrict__ pout, float* __restrict__ x1) {
  __shared__ float red[4 * 64];
  const int b = blockIdx.x >> 3, cb = blockIdx.x & 7;
  const int tid = threadIdx.x, col = tid & 63, g = tid >> 6;
  const int n = cb * 64 + col;
  float pv[8];
#pragma unroll
  for (int hh = 0; hh < 8; ++hh) pv[hh] = pout[((size_t)(g * 8 + hh) * NB + b) * DM + n];
  const float xv = bf2f(x[b * DM + n]);
  const float ga = mod[b * NMOD + 2 * DM + n];
  float acc = 0.f;
#pragma unroll
  for (int hh = 0; hh < 8; ++hh) acc += pv[hh];
  red[g * 64 + col] = acc;
  __syncthreads();
  if (tid < 64) {
    const float attn = rbf(red[col] + red[64 + col] + red[128 + col] + red[192 + col]);
    x1[b * DM + n] = rbf(xv + rbf(ga * attn));
  }
}

__global__ void __launch_bounds__(512) k_ffn(const float* __restrict__ x1, const float* __restrict__ mod,
                                             const unsigned short* __restrict__ wg, const unsigned short* __restrict__ wu,
                                             const unsigned short* __restrict__ wd, float* __restrict__ p2) {
  __shared__ float h2[NB * DM];
  __shared__ float red[8 * 128];
  __shared__ float act[NB * 16];
  __shared__ float red2[8 * NB * DM];
  const int tid = threadIdx.x, lane = tid & 63, wave = tid >> 6;
  const int wgid = (blockIdx.x & 7) * 14 + (blockIdx.x >> 3);
  const int f0 = wgid * 16;
  const int rr = lane >> 1, cc = lane & 1;
  float xv[8], mcb[8], msb[8];
  if (wave < NB) {
#pragma unroll
    for (int k = 0; k < 8; ++k) xv[k] = x1[wave * DM + lane * 8 + k];
#pragma unroll
    for (int k = 0; k < 8; ++k) { mcb[k] = mod[wave * NMOD + 4 * DM + lane * 8 + k]; msb[k] = mod[wave * NMOD + 3 * DM + lane * 8 + k]; }
  }
  u32x4 g4[2], u4[2];
#pragma unroll
  for (int p = 0; p < 2; ++p) {
    const int row = p * 256 + wave * 32 + rr;
    g4[p] = ld_nt(wg + (size_t)row * FF + f0 + cc * 8);
    u4[p] = ld_nt(wu + (size_t)row * FF + f0 + cc * 8);
  }
  u32x4 d4[2];
#pragma unroll
  for (int i = 0; i < 2; ++i) d4[i] = ld_nt(wd + (size_t)(f0 + wave * 2 + i) * DM + lane * 8);

  if (wave < NB) {
    const int b = wave;
    float s = 0.f;
#pragma unroll
    for (int k = 0; k < 8; ++k) s += xv[k];
    s = wave_sum(s);
    const float mean = s * (1.0f / DM);
    float s2 = 0.f;
#pragma unroll
    for (int k = 0; k < 8; ++k) { const float d = xv[k] - mean; s2 += d * d; }
    s2 = wave_sum(s2);
    const float var = s2 * (1.0f / DM);
    const float mean_b = rbf(mean);
    const float rden = rbf(sqrtf(rbf(rbf(var) + 1e-5f)));
#pragma unroll
    for (int k = 0; k < 8; ++k) {
      const int n = lane * 8 + k;
      h2[b * DM + n] = ln_mod_elem(xv[k], mean_b, rden, mcb[k], msb[k]);
    }
  }
  __syncthreads();
  float acc[64];
#pragma unroll
  for (int j = 0; j < 64; ++j) acc[j] = 0.f;
#pragma unroll
  for (int p = 0; p < 2; ++p) {
    const int row = p * 256 + wave * 32 + rr;
    float gf[8], uf[8];
#pragma unroll
    for (int k = 0; k < 4; ++k) {
      gf[2 * k] = bf_lo(g4[p][k]); gf[2 * k + 1] = bf_hi(g4[p][k]);
      uf[2 * k] = bf_lo(u4[p][k]); uf[2 * k + 1] = bf_hi(u4[p][k]);
    }
#pragma unroll
    for (int b = 0; b < NB; ++b) {
      const float hv = h2[b * DM + row];
#pragma unroll
      for (int j = 0; j < 8; ++j) {
        acc[b * 8 + j] = fmaf(hv, gf[j], acc[b * 8 + j]);
        acc[32 + b * 8 + j] = fmaf(hv, uf[j], acc[32 + b * 8 + j]);
      }
    }
  }
  tr_reduce_step<64>(acc, lane, 32);
  tr_reduce_step<32>(acc, lane, 16);
  tr_reduce_step<16>(acc, lane, 8);
  tr_reduce_step<8>(acc, lane, 4);
  tr_reduce_step<4>(acc, lane, 2);
  {
    const int hi = ((lane >> 5) & 1) * 32 + ((lane >> 4) & 1) * 16 + ((lane >> 3) & 1) * 8 + ((lane >> 2) & 1) * 4 + ((lane >> 1) & 1) * 2;
#pragma unroll
    for (int t = 0; t < 2; ++t) {
      const int j = hi + t;
      const int mat = j >> 5, b = (j >> 3) & 3, jj = j & 7;
      red[wave * 128 + mat * 64 + b * 16 + cc * 8 + jj] = acc[t];
    }
  }
  __syncthreads();
  if (tid < 64) {
    float g = 0.f, u = 0.f;
#pragma unroll
    for (int wv = 0; wv < 8; ++wv) { g += red[wv * 128 + tid]; u += red[wv * 128 + 64 + tid]; }
    g = rbf(g); u = rbf(u);
    const float inner = 0.7978845608028654f * (g + 0.044715f * g * g * g);
    const float ge = rbf(0.5f * g * (1.0f + tanhf(inner)));
    act[tid] = rbf(ge * u);
  }
  __syncthreads();
  {
#pragma unroll
    for (int b = 0; b < NB; ++b) {
      float pa[8];
#pragma unroll
      for (int j = 0; j < 8; ++j) pa[j] = 0.f;
#pragma unroll
      for (int i = 0; i < 2; ++i) {
        const float av = act[b * 16 + wave * 2 + i];
#pragma unroll
        for (int k = 0; k < 4; ++k) {
          pa[2 * k] = fmaf(av, bf_lo(d4[i][k]), pa[2 * k]);
          pa[2 * k + 1] = fmaf(av, bf_hi(d4[i][k]), pa[2 * k + 1]);
        }
      }
#pragma unroll
      for (int j = 0; j < 8; ++j) red2[(wave * NB + b) * DM + lane * 8 + j] = pa[j];
    }
    __syncthreads();
#pragma unroll
    for (int b = 0; b < NB; ++b) {
      const int n = tid;
      float s = 0.f;
#pragma unroll
      for (int wv = 0; wv < 8; ++wv) s += red2[(wv * NB + b) * DM + n];
      p2[((size_t)wgid * NB + b) * DM + n] = s;
    }
  }
}

__global__ void __launch_bounds__(256) k_red2(const float* __restrict__ x1, const float* __restrict__ mod, const float* __restrict__ p2,
                                              __hip_bfloat16* __restrict__ out) {
  __shared__ float red[4 * 64];
  const int b = blockIdx.x >> 3, cb = blockIdx.x & 7;
  const int tid = threadIdx.x, col = tid & 63, g = tid >> 6;
  const int n = cb * 64 + col;
  float pv[28];
#pragma unroll
  for (int i = 0; i < 28; ++i) pv[i] = p2[((size_t)(g * 28 + i) * NB + b) * DM + n];
  const float xv = x1[b * DM + n];
  const float gb = mod[b * NMOD + 5 * DM + n];
  float acc = 0.f;
#pragma unroll
  for (int i = 0; i < 28; ++i) acc += pv[i];
  red[g * 64 + col] = acc;
  __syncthreads();
  if (tid < 64) {
    const float y = rbf(red[col] + red[64 + col] + red[128 + col] + red[192 + col]);
    out[b * DM + n] = __float2bfloat16(xv + rbf(gb * y));
  }
}

extern "C" void kernel_launch(void* const* d_in, const int* in_sizes, int n_in, void* d_out, int out_size, void* d_ws, size_t ws_size,
                              hipStream_t stream) {
  const unsigned short* x = (const unsigned short*)d_in[0];
  const unsigned short* kc = (const unsigned short*)d_in[1];
  const unsigned short* vc = (const unsigned short*)d_in[2];
  const unsigned short* wq = (const unsigned short*)d_in[3];
  const unsigned short* wo = (const unsigned short*)d_in[4];
  const unsigned short* wg = (const unsigned short*)d_in[6];
  const unsigned short* wu = (const unsigned short*)d_in[7];
  const unsigned short* wd = (const unsigned short*)d_in[8];
  const unsigned short* cv = (const unsigned short*)d_in[9];
  const unsigned short* wmod = (const unsigned short*)d_in[10];

  constexpr int WAVES = 8;
  float* ws = (float*)d_ws;
  float* mod = ws;
  float* x1 = mod + NB * NMOD;
  float* pout = x1 + NB * DM;
  float* p2 = pout + (size_t)NH * NB * DM;
  u64* qg = (u64*)(p2 + (size_t)112 * NB * DM);
  u64* og = qg + (size_t)NH * NB * DH;
  const int gran16 = (NH * NB * DH + NH * GRP * OREC) * 8 / 16;

  k_mod<<<96, 512, 0, stream>>>(wmod, cv, mod, (u32x4*)qg, gran16);
  k_attn<WAVES><<<NH * GRP, WAVES * 64, 0, stream>>>(x, kc, vc, wq, wo, mod, pout, qg, og);
  k_red1<<<NB * 8, 256, 0, stream>>>(x, mod, pout, x1);
  k_ffn<<<112, 512, 0, stream>>>(x1, mod, wg, wu, wd, p2);
  k_red2<<<NB * 8, 256, 0, stream>>>(x1, mod, p2, (__hip_bfloat16*)d_out);
}
```

```cpp
#include <hip/hip_runtime.h>
#include <hip/hip_bf16.h>
#include <stdint.h>
#include <type_traits>

typedef unsigned int u32;
typedef __attribute__((ext_vector_type(4))) u32 u32x4;
typedef __attribute__((ext_vector_type(2))) __bf16 bf16x2_t;

#define NB 4
#define DM 512
#define NH 32
#define SK 4096
#define DH 128
#define FF 1792
#define NMOD 3072

__device__ __forceinline__ float bf_lo(u32 u) { return __uint_as_float(u << 16); }
__device__ __forceinline__ float bf_hi(u32 u) { return __uint_as_float(u & 0xffff0000u); }
__device__ __forceinline__ float bf2f(unsigned short u) { return __uint_as_float(((u32)u) << 16); }
__device__ __forceinline__ float rbf(float x) {
  u32 u = __float_as_uint(x);
  u += 0x7fffu + ((u >> 16) & 1u);
  return __uint_as_float(u & 0xffff0000u);
}
__device__ __forceinline__ u32 pack_bf16(float lo, float hi) {
  return (__float_as_uint(lo) >> 16) | (__float_as_uint(hi) & 0xffff0000u);
}
__device__ __forceinline__ float dot2bf(u32 a, u32 b, float acc) {
  return __builtin_amdgcn_fdot2_f32_bf16(__builtin_bit_cast(bf16x2_t, a), __builtin_bit_cast(bf16x2_t, b), acc, false);
}
template <int CTRL> __device__ __forceinline__ float dppf(float x) {
  return __builtin_bit_cast(float, __builtin_amdgcn_mov_dpp(__builtin_bit_cast(int, x), CTRL, 0xf, 0xf, true));
}
__device__ __forceinline__ float row16_sum(float x) {
  x += dppf<0x128>(x);
  x += dppf<0x124>(x);
  x += dppf<0x122>(x);
  x += dppf<0x121>(x);
  return x;
}
__device__ __forceinline__ float wave_sum(float x) {
  x = row16_sum(x);
  x += __shfl_xor(x, 16, 64);
  x += __shfl_xor(x, 32, 64);
  return x;
}
__device__ __forceinline__ u32x4 ld_nt(const void* p) { return __builtin_nontemporal_load(reinterpret_cast<const u32x4*>(p)); }
__device__ __forceinline__ u32x4 ld16(const void* p) { return *reinterpret_cast<const u32x4*>(p); }

typedef unsigned long long u64;
__device__ __forceinline__ void st_granule(u64* p, float v) {
  __hip_atomic_store(p, (1ull << 32) | (u64)__float_as_uint(v), __ATOMIC_RELAXED, __HIP_MEMORY_SCOPE_AGENT);
}
__device__ __forceinline__ u64 ld_granule(const u64* p) { return __hip_atomic_load(p, __ATOMIC_RELAXED, __HIP_MEMORY_SCOPE_AGENT); }

template <int N> __device__ __forceinline__ void tr_reduce_step(float (&v)[64], int lane, int mask) {
  const bool up = (lane & mask) != 0;
#pragma unroll
  for (int j = 0; j < N / 2; ++j) {
    const float send = up ? v[j] : v[j + N / 2];
    const float keep = up ? v[j + N / 2] : v[j];
    v[j] = keep + __shfl_xor(send, mask, 64);
  }
}

__device__ __forceinline__ float ln_mod_elem(float xv, float mean_b, float rden, float sc, float sh) {
  const float xm = rbf(xv - mean_b);
  const float nrm = rbf(xm / rden);
  const float t = rbf(nrm * rbf(1.0f + sc));
  return rbf(t + sh);
}

__global__ void __launch_bounds__(512) k_mod(const unsigned short* __restrict__ wmod, const unsigned short* __restrict__ cvec,
                                             float* __restrict__ mod, u32x4* __restrict__ gran, int gran16) {
  __shared__ float sc[NB * DM];
  __shared__ float red[8 * 128];
  const int tid = threadIdx.x, lane = tid & 63, wave = tid >> 6;
  const int rr = lane >> 2, cc = lane & 3;
  const int col0 = blockIdx.x * 32 + cc * 8;
  float cvv[4];
#pragma unroll
  for (int i = 0; i < 4; ++i) cvv[i] = bf2f(cvec[tid + i * 512]);
  u32x4 w[4];
#pragma unroll
  for (int p = 0; p < 4; ++p) {
    const int row = p * 128 + wave * 16 + rr;
    w[p] = ld_nt(wmod + (size_t)row * NMOD + col0);
  }
#pragma unroll
  for (int i = 0; i < 4; ++i) sc[tid + i * 512] = rbf(cvv[i] / (1.0f + __expf(-cvv[i])));
  for (int i = blockIdx.x * 512 + tid; i < gran16; i += gridDim.x * 512) gran[i] = u32x4{0u, 0u, 0u, 0u};
  __syncthreads();
  float acc[64];
#pragma unroll
  for (int j = 0; j < 32; ++j) acc[j] = 0.f;
#pragma unroll
  for (int p = 0; p < 4; ++p) {
    const int row = p * 128 + wave * 16 + rr;
    float wf[8];
#pragma unroll
    for (int k = 0; k < 4; ++k) { wf[2 * k] = bf_lo(w[p][k]); wf[2 * k + 1] = bf_hi(w[p][k]); }
#pragma unroll
    for (int b = 0; b < NB; ++b) {
      const float s = sc[b * DM + row];
#pragma unroll
      for (int j = 0; j < 8; ++j) acc[b * 8 + j] = fmaf(s, wf[j], acc[b * 8 + j]);
    }
  }
  tr_reduce_step<32>(acc, lane, 32);
  tr_reduce_step<16>(acc, lane, 16);
  tr_reduce_step<8>(acc, lane, 8);
  tr_reduce_step<4>(acc, lane, 4);
  {
    const int hi = ((lane >> 5) & 1) * 16 + ((lane >> 4) & 1) * 8 + ((lane >> 3) & 1) * 4 + ((lane >> 2) & 1) * 2;
#pragma unroll
    for (int t = 0; t < 2; ++t) {
      const int j = hi + t;
      const int b = j >> 3, jj = j & 7;
      red[wave * 128 + b * 32 + cc * 8 + jj] = acc[t];
    }
  }
  __syncthreads();
  if (tid < 128) {
    float s = 0.f;
#pragma unroll
    for (int wv = 0; wv < 8; ++wv) s += red[wv * 128 + tid];
    const int b = tid >> 5, col = tid & 31;
    mod[b * NMOD + blockIdx.x * 32 + col] = rbf(s);
  }
}

#define GRP 8
#define OREC 132
template <int WAVES>
__global__ void __launch_bounds__(WAVES * 64) k_attn(const unsigned short* __restrict__ x, const unsigned short* __restrict__ kc,
                                                     const unsigned short* __restrict__ vc, const unsigned short* __restrict__ wq,
                                                     const unsigned short* __restrict__ wo, const float* __restrict__ mod,
                                                     float* __restrict__ pout, u64* __restrict__ qg, u64* __restrict__ og) {
  constexpr int SPLITS = 2;
  constexpr int NT = WAVES * 64;
  constexpr int KEYS_WG = SK / SPLITS;
  constexpr int TILES = KEYS_WG / (16 * WAVES);
  constexpr int NG = WAVES * 4;
  __shared__ float hvec[NB * DM];
  __shared__ float qred[NG * NB * DH];
  __shared__ float gml[NG * 2];
  __shared__ float ofin[NB * DH];
  __shared__ int tile_ctr;

  const int tid = threadIdx.x, lane = tid & 63, wave = tid >> 6;
  const int r = lane >> 4, c = lane & 15;
  const int h = blockIdx.x / GRP, member = blockIdx.x % GRP;
  const int b = member >> 1, split = member & 1;
  const int bh = b * NH + h;
  const bool leader = (member == 0);
  if (tid == 0) tile_ctr = 2 * WAVES;

  const unsigned short* kbase = kc + ((size_t)bh * SK + (size_t)split * KEYS_WG) * DH + (size_t)r * DH + c * 8;
  const unsigned short* vbase = vc + ((size_t)bh * SK + (size_t)split * KEYS_WG) * DH + (size_t)r * DH + c * 8;

  auto produce_q = [&](auto chunk_tag) {
    constexpr int QP = DM / (4 * WAVES);
    constexpr int CH = decltype(chunk_tag)::value;
    float xv[8], mca[8], msa[8];
    if (wave < NB) {
      const u32x4 xx = ld16(x + wave * DM + lane * 8);
#pragma unroll
      for (int k = 0; k < 4; ++k) { xv[2 * k] = bf_lo(xx[k]); xv[2 * k + 1] = bf_hi(xx[k]); }
#pragma unroll
      for (int k = 0; k < 8; ++k) { mca[k] = mod[wave * NMOD + DM + lane * 8 + k]; msa[k] = mod[wave * NMOD + lane * 8 + k]; }
    }
    u32x4 wqv[CH];
#pragma unroll
    for (int p = 0; p < CH; ++p) {
      const int row = p * (4 * WAVES) + wave * 4 + r;
      wqv[p] = ld_nt(wq + (size_t)row * (NH * DH) + h * DH + c * 8);
    }
    if (wave < NB) {
      float s = 0.f, s2 = 0.f;
#pragma unroll
      for (int k = 0; k < 8; ++k) s += xv[k];
      s = wave_sum(s);
      const float mean = s * (1.0f / DM);
#pragma unroll
      for (int k = 0; k < 8; ++k) { const float d = xv[k] - mean; s2 += d * d; }
      s2 = wave_sum(s2);
      const float var = s2 * (1.0f / DM);
      const float mean_b = rbf(mean);
      const float rden = rbf(sqrtf(rbf(rbf(var) + 1e-5f)));
#pragma unroll
      for (int k = 0; k < 8; ++k) hvec[wave * DM + lane * 8 + k] = ln_mod_elem(xv[k], mean_b, rden, mca[k], msa[k]);
    }
    __syncthreads();
    float qa[NB][8];
#pragma unroll
    for (int bb = 0; bb < NB; ++bb)
#pragma unroll
      for (int j = 0; j < 8; ++j) qa[bb][j] = 0.f;
#pragma unroll 1
    for (int p0 = 0; p0 < QP; p0 += CH) {
      if (p0 > 0) {
#pragma unroll
        for (int p = 0; p < CH; ++p) {
          const int row = (p0 + p) * (4 * WAVES) + wave * 4 + r;
          wqv[p] = ld_nt(wq + (size_t)row * (NH * DH) + h * DH + c * 8);
        }
      }
#pragma unroll
      for (int p = 0; p < CH; ++p) {
        const int row = (p0 + p) * (4 * WAVES) + wave * 4 + r;
#pragma unroll
        for (int bb = 0; bb < NB; ++bb) {
          const float hv = hvec[bb * DM + row];
#pragma unroll
          for (int k = 0; k < 4; ++k) {
            qa[bb][2 * k] = fmaf(hv, bf_lo(wqv[p][k]), qa[bb][2 * k]);
            qa[bb][2 * k + 1] = fmaf(hv, bf_hi(wqv[p][k]), qa[bb][2 * k + 1]);
          }
        }
      }
    }
#pragma unroll
    for (int bb = 0; bb < NB; ++bb)
#pragma unroll
      for (int j = 0; j < 8; ++j) qred[((wave * 4 + r) * NB + bb) * DH + c * 8 + j] = qa[bb][j];
    __syncthreads();
    for (int i = tid; i < NB * DH; i += NT) {
      float s = 0.f;
      for (int g = 0; g < NG; ++g) s += qred[g * NB * DH + i];
      st_granule(qg + (size_t)h * NB * DH + i, rbf(s));
    }
    __syncthreads();
  };

  if (leader) produce_q(std::integral_constant<int, DM / (4 * WAVES)>{});

  u32x4 kA[4], vA[4], kB[4], vB[4];
  {
    const size_t off = (size_t)wave * 16 * DH;
#pragma unroll
    for (int i = 0; i < 4; ++i) kA[i] = ld_nt(kbase + off + (size_t)i * 4 * DH);
#pragma unroll
    for (int i = 0; i < 4; ++i) vA[i] = ld_nt(vbase + off + (size_t)i * 4 * DH);
    const size_t off2 = (size_t)(WAVES + wave) * 16 * DH;
#pragma unroll
    for (int i = 0; i < 4; ++i) kB[i] = ld_nt(kbase + off2 + (size_t)i * 4 * DH);
#pragma unroll
    for (int i = 0; i < 4; ++i) vB[i] = ld_nt(vbase + off2 + (size_t)i * 4 * DH);
  }

  u32 qpk[4];
  {
    const u64* qp = qg + ((size_t)h * NB + b) * DH;
    for (;;) {
      int ok = 1;
      if (wave == 0) {
        u64 g0 = 0, g1 = 0;
        for (int spin = 0; spin < 4000; ++spin) {
          g0 = ld_granule(qp + 2 * lane); g1 = ld_granule(qp + 2 * lane + 1);
          ok = __all(((g0 & g1) >> 32) == 1ull);
          if (ok) break;
          __builtin_amdgcn_s_sleep(8);
        }
        ofin[2 * lane] = __uint_as_float((u32)g0);
        ofin[2 * lane + 1] = __uint_as_float((u32)g1);
      }
      if (__syncthreads_and(ok)) break;
      produce_q(std::integral_constant<int, 1>{});
    }
#pragma unroll
    for (int k = 0; k < 4; ++k) qpk[k] = pack_bf16(ofin[c * 8 + 2 * k], ofin[c * 8 + 2 * k + 1]);
  }

  const float SCALE = 0.08838834764831845f;
  const float LOG2E = 1.4426950408889634f;
  float m_run = -INFINITY, l_run = 0.f;
  float o[8];
#pragma unroll
  for (int j = 0; j < 8; ++j) o[j] = 0.f;

  auto compute = [&](const u32x4 (&kt)[4], const u32x4 (&vt)[4]) {
    float sc[4];
#pragma unroll
    for (int i = 0; i < 4; ++i) {
      float d = 0.f;
#pragma unroll
      for (int k = 0; k < 4; ++k) d = dot2bf(kt[i][k], qpk[k], d);
      d = row16_sum(d);
      sc[i] = rbf(rbf(d) * SCALE) * LOG2E;
    }
    const float mt = fmaxf(fmaxf(sc[0], sc[1]), fmaxf(sc[2], sc[3]));
    const float m_new = fmaxf(m_run, mt);
    const float alpha = __builtin_amdgcn_exp2f(m_run - m_new);
    float p[4];
#pragma unroll
    for (int i = 0; i < 4; ++i) p[i] = __builtin_amdgcn_exp2f(sc[i] - m_new);
    l_run = fmaf(l_run, alpha, (p[0] + p[1]) + (p[2] + p[3]));
    m_run = m_new;
#pragma unroll
    for (int j = 0; j < 8; ++j) o[j] *= alpha;
#pragma unroll
    for (int i = 0; i < 4; ++i) {
#pragma unroll
      for (int k = 0; k < 4; ++k) {
        o[2 * k] = fmaf(p[i], bf_lo(vt[i][k]), o[2 * k]);
        o[2 * k + 1] = fmaf(p[i], bf_hi(vt[i][k]), o[2 * k + 1]);
      }
    }
  };
  auto load_tile = [&](int t, u32x4 (&kt)[4], u32x4 (&vt)[4]) {
    const size_t off = (size_t)t * 16 * DH;
#pragma unroll
    for (int i = 0; i < 4; ++i) kt[i] = ld_nt(kbase + off + (size_t)i * 4 * DH);
#pragma unroll
    for (int i = 0; i < 4; ++i) vt[i] = ld_nt(vbase + off + (size_t)i * 4 * DH);
  };
  auto grab = [&]() -> int {
    int t = 0;
    if (lane == 0) t = atomicAdd(&tile_ctr, 1);
    return __builtin_amdgcn_readfirstlane(t);
  };
  constexpr int NTILE = KEYS_WG / 16;
  {
    bool vA_ok = true, vB_ok = true;
    while (vA_ok || vB_ok) {
      if (vA_ok) {
        compute(kA, vA);
        const int t = grab();
        vA_ok = t < NTILE;
        if (vA_ok) load_tile(t, kA, vA);
      }
      if (vB_ok) {
        compute(kB, vB);
        const int t = grab();
        vB_ok = t < NTILE;
        if (vB_ok) load_tile(t, kB, vB);
      }
    }
  }
  constexpr int RW = DH / WAVES;
  u32x4 wv[RW];
  if (leader) {
#pragma unroll
    for (int i = 0; i < RW; ++i) wv[i] = ld_nt(wo + (size_t)(h * DH + wave * RW + i) * DM + lane * 8);
  }

  __syncthreads();
  {
    const int g = wave * 4 + r;
#pragma unroll
    for (int j = 0; j < 8; ++j) qred[g * DH + c * 8 + j] = o[j];
    if (c == 0) { gml[g * 2] = m_run; gml[g * 2 + 1] = l_run; }
  }
  __syncthreads();
  float myM = 0.f, myL = 0.f, myO = 0.f;
  if (tid < DH) {
    float M = -INFINITY;
    for (int g = 0; g < NG; ++g) M = fmaxf(M, gml[g * 2]);
    float L = 0.f, od = 0.f;
    for (int g = 0; g < NG; ++g) {
      const float wt = __builtin_amdgcn_exp2f(gml[g * 2] - M);
      L = fmaf(wt, gml[g * 2 + 1], L);
      od = fmaf(wt, qred[g * DH + tid], od);
    }
    myM = M; myL = L; myO = od;
    u64* rec = og + ((size_t)h * GRP + member) * OREC;
    st_granule(rec + 4 + tid, od);
    if (tid == 0) { st_granule(rec, M); st_granule(rec + 1, L); }
  }
  if (!leader) return;

  {
    for (int i = tid; i < NB * DH; i += NT) {
      const int bb = i / DH, d = i % DH;
      const u64* r0 = og + ((size_t)h * GRP + bb * 2) * OREC;
      const u64* r1 = r0 + OREC;
      float m0, l0, o0, m1, l1, o1;
      for (int spin = 0; spin < (1 << 24); ++spin) {
        const u64 a0 = ld_granule(r0), a1 = ld_granule(r0 + 1), a2 = ld_granule(r0 + 4 + d);
        const u64 b0 = ld_granule(r1), b1 = ld_granule(r1 + 1), b2 = ld_granule(r1 + 4 + d);
        m0 = __uint_as_float((u32)a0); l0 = __uint_as_float((u32)a1); o0 = __uint_as_float((u32)a2);
        m1 = __uint_as_float((u32)b0); l1 = __uint_as_float((u32)b1); o1 = __uint_as_float((u32)b2);
        if (((a0 & a1 & a2 & b0 & b1 & b2) >> 32) == 1ull) break;
        __builtin_amdgcn_s_sleep(1);
      }
      const float M = fmaxf(m0, m1);
      const float w0 = __builtin_amdgcn_exp2f(m0 - M), w1 = __builtin_amdgcn_exp2f(m1 - M);
      const float L = fmaf(w0, l0, w1 * l1);
      ofin[i] = fmaf(w0, o0, w1 * o1) / L;
    }
    __syncthreads();
#pragma unroll
    for (int bb = 0; bb < NB; ++bb) {
      float pa[8];
#pragma unroll
      for (int j = 0; j < 8; ++j) pa[j] = 0.f;
#pragma unroll
      for (int i = 0; i < RW; ++i) {
        const float ov = ofin[bb * DH + wave * RW + i];
#pragma unroll
        for (int k = 0; k < 4; ++k) {
          pa[2 * k] = fmaf(ov, bf_lo(wv[i][k]), pa[2 * k]);
          pa[2 * k + 1] = fmaf(ov, bf_hi(wv[i][k]), pa[2 * k + 1]);
        }
      }
#pragma unroll
      for (int j = 0; j < 8; ++j) qred[(wave * NB + bb) * DM + lane * 8 + j] = pa[j];
    }
    __syncthreads();
    for (int i = tid; i < NB * DM; i += NT) {
      const int bb = i / DM, n = i % DM;
      float s = 0.f;
#pragma unroll
      for (int w2 = 0; w2 < WAVES; ++w2) s += qred[(w2 * NB + bb) * DM + n];
      pout[((size_t)h * NB + bb) * DM + n] = s;
    }
  }
}

__global__ void __launch_bounds__(256) k_red1(const unsigned short* __restrict__ x, const float* __restrict__ mod,
                                              const float* __restrict__ pout, float* __restrict__ x1) {
  __shared__ float red[4 * 64];
  const int b = blockIdx.x >> 3, cb = blockIdx.x & 7;
  const int tid = threadIdx.x, col = tid & 63, g = tid >> 6;
  const int n = cb * 64 + col;
  float pv[8];
#pragma unroll
  for (int hh = 0; hh < 8; ++hh) pv[hh] = pout[((size_t)(g * 8 + hh) * NB + b) * DM + n];
  const float xv = bf2f(x[b * DM + n]);
  const float ga = mod[b * NMOD + 2 * DM + n];
  float acc = 0.f;
#pragma unroll
  for (int hh = 0; hh < 8; ++hh) acc += pv[hh];
  red[g * 64 + col] = acc;
  __syncthreads();
  if (tid < 64) {
    const float attn = rbf(red[col] + red[64 + col] + red[128 + col] + red[192 + col]);
    x1[b * DM + n] = rbf(xv + rbf(ga * attn));
  }
}

__global__ void __launch_bounds__(512) k_ffn(const unsigned short* __restrict__ x, const float* __restrict__ pout, u64* __restrict__ xg,
                                             float* __restrict__ x1, const float* __restrict__ mod,
                                             const unsigned short* __restrict__ wg, const unsigned short* __restrict__ wu,
                                             const unsigned short* __restrict__ wd, float* __restrict__ p2) {
  __shared__ float h2[NB * DM];
  __shared__ float red[8 * 128];
  __shared__ float act[NB * 16];
  __shared__ float red2[8 * NB * DM];
  const int tid = threadIdx.x, lane = tid & 63, wave = tid >> 6;
  const int wgid = (blockIdx.x & 7) * 14 + (blockIdx.x >> 3);
  const int f0 = wgid * 16;
  const int rr = lane >> 1, cc = lane & 1;
  if (blockIdx.x < NB * 8) {
    const int rb = blockIdx.x >> 3, cb = blockIdx.x & 7, col = tid & 63, g = tid >> 6;
    const int n = cb * 64 + col;
    float pv[4];
#pragma unroll
    for (int hh = 0; hh < 4; ++hh) pv[hh] = pout[((size_t)(g * 4 + hh) * NB + rb) * DM + n];
    red[g * 64 + col] = (pv[0] + pv[1]) + (pv[2] + pv[3]);
    __syncthreads();
    if (tid < 64) {
      float a = 0.f;
#pragma unroll
      for (int gg = 0; gg < 8; ++gg) a += red[gg * 64 + col];
      const float v = rbf(bf2f(x[rb * DM + n]) + rbf(mod[rb * NMOD + 2 * DM + n] * rbf(a)));
      x1[rb * DM + n] = v;
      st_granule(xg + rb * DM + n, v);
    }
    __syncthreads();
  }
  float xv[8], mcb[8], msb[8];
  if (wave < NB) {
#pragma unroll
    for (int k = 0; k < 8; ++k) { mcb[k] = mod[wave * NMOD + 4 * DM + lane * 8 + k]; msb[k] = mod[wave * NMOD + 3 * DM + lane * 8 + k]; }
  }
  u32x4 g4[2], u4[2];
#pragma unroll
  for (int p = 0; p < 2; ++p) {
    const int row = p * 256 + wave * 32 + rr;
    g4[p] = ld_nt(wg + (size_t)row * FF + f0 + cc * 8);
    u4[p] = ld_nt(wu + (size_t)row * FF + f0 + cc * 8);
  }
  u32x4 d4[2];
#pragma unroll
  for (int i = 0; i < 2; ++i) d4[i] = ld_nt(wd + (size_t)(f0 + wave * 2 + i) * DM + lane * 8);

  if (wave < NB) {
    const int b = wave;
    for (int spin = 0; spin < (1 << 24); ++spin) {
      bool ok = true;
#pragma unroll
      for (int k = 0; k < 8; ++k) { const u64 gq = ld_granule(xg + b * DM + lane * 8 + k); xv[k] = __uint_as_float((u32)gq); ok = ok && ((gq >> 32) == 1ull); }
      if (__all(ok)) break;
      __builtin_amdgcn_s_sleep(2);
    }
    float s = 0.f;
#pragma unroll
    for (int k = 0; k < 8; ++k) s += xv[k];
    s = wave_sum(s);
    const float mean = s * (1.0f / DM);
    float s2 = 0.f;
#pragma unroll
    for (int k = 0; k < 8; ++k) { const float d = xv[k] - mean; s2 += d * d; }
    s2 = wave_sum(s2);
    const float var = s2 * (1.0f / DM);
    const float mean_b = rbf(mean);
    const float rden = rbf(sqrtf(rbf(rbf(var) + 1e-5f)));
#pragma unroll
    for (int k = 0; k < 8; ++k) {
      const int n = lane * 8 + k;
      h2[b * DM + n] = ln_mod_elem(xv[k], mean_b, rden, mcb[k], msb[k]);
    }
  }
  __syncthreads();
  float acc[64];
#pragma unroll
  for (int j = 0; j < 64; ++j) acc[j] = 0.f;
#pragma unroll
  for (int p = 0; p < 2; ++p) {
    const int row = p * 256 + wave * 32 + rr;
    float gf[8], uf[8];
#pragma unroll
    for (int k = 0; k < 4; ++k) {
      gf[2 * k] = bf_lo(g4[p][k]); gf[2 * k + 1] = bf_hi(g4[p][k]);
      uf[2 * k] = bf_lo(u4[p][k]); uf[2 * k + 1] = bf_hi(u4[p][k]);
    }
#pragma unroll
    for (int b = 0; b < NB; ++b) {
      const float hv = h2[b * DM + row];
#pragma unroll
      for (int j = 0; j < 8; ++j) {
        acc[b * 8 + j] = fmaf(hv, gf[j], acc[b * 8 + j]);
        acc[32 + b * 8 + j] = fmaf(hv, uf[j], acc[32 + b * 8 + j]);
      }
    }
  }
  tr_reduce_step<64>(acc, lane, 32);
  tr_reduce_step<32>(acc, lane, 16);
  tr_reduce_step<16>(acc, lane, 8);
  tr_reduce_step<8>(acc, lane, 4);
  tr_reduce_step<4>(acc, lane, 2);
  {
    const int hi = ((lane >> 5) & 1) * 32 + ((lane >> 4) & 1) * 16 + ((lane >> 3) & 1) * 8 + ((lane >> 2) & 1) * 4 + ((lane >> 1) & 1) * 2;
#pragma unroll
    for (int t = 0; t < 2; ++t) {
      const int j = hi + t;
      const int mat = j >> 5, b = (j >> 3) & 3, jj = j & 7;
      red[wave * 128 + mat * 64 + b * 16 + cc * 8 + jj] = acc[t];
    }
  }
  __syncthreads();
  if (tid < 64) {
    float g = 0.f, u = 0.f;
#pragma unroll
    for (int wv = 0; wv < 8; ++wv) { g += red[wv * 128 + tid]; u += red[wv * 128 + 64 + tid]; }
    g = rbf(g); u = rbf(u);
    const float inner = 0.7978845608028654f * (g + 0.044715f * g * g * g);
    const float ge = rbf(0.5f * g * (1.0f + tanhf(inner)));
    act[tid] = rbf(ge * u);
  }
  __syncthreads();
  {
#pragma unroll
    for (int b = 0; b < NB; ++b) {
      float pa[8];
#pragma unroll
      for (int j = 0; j < 8; ++j) pa[j] = 0.f;
#pragma unroll
      for (int i = 0; i < 2; ++i) {
        const float av = act[b * 16 + wave * 2 + i];
#pragma unroll
        for (int k = 0; k < 4; ++k) {
          pa[2 * k] = fmaf(av, bf_lo(d4[i][k]), pa[2 * k]);
          pa[2 * k + 1] = fmaf(av, bf_hi(d4[i][k]), pa[2 * k + 1]);
        }
      }
#pragma unroll
      for (int j = 0; j < 8; ++j) red2[(wave * NB + b) * DM + lane * 8 + j] = pa[j];
    }
    __syncthreads();
#pragma unroll
    for (int b = 0; b < NB; ++b) {
      const int n = tid;
      float s = 0.f;
#pragma unroll
      for (int wv = 0; wv < 8; ++wv) s += red2[(wv * NB + b) * DM + n];
      p2[((size_t)wgid * NB + b) * DM + n] = s;
    }
  }
}

__global__ void __launch_bounds__(256) k_red2(const float* __restrict__ x1, const float* __restrict__ mod, const float* __restrict__ p2,
                                              __hip_bfloat16* __restrict__ out) {
  __shared__ float red[4 * 64];
  const int b = blockIdx.x >> 3, cb = blockIdx.x & 7;
  const int tid = threadIdx.x, col = tid & 63, g = tid >> 6;
  const int n = cb * 64 + col;
  float pv[28];
#pragma unroll
  for (int i = 0; i < 28; ++i) pv[i] = p2[((size_t)(g * 28 + i) * NB + b) * DM + n];
  const float xv = x1[b * DM + n];
  const float gb = mod[b * NMOD + 5 * DM + n];
  float acc = 0.f;
#pragma unroll
  for (int i = 0; i < 28; ++i) acc += pv[i];
  red[g * 64 + col] = acc;
  __syncthreads();
  if (tid < 64) {
    const float y = rbf(red[col] + red[64 + col] + red[128 + col] + red[192 + col]);
    out[b * DM + n] = __float2bfloat16(xv + rbf(gb * y));
  }
}

extern "C" void kernel_launch(void* const* d_in, const int* in_sizes, int n_in, void* d_out, int out_size, void* d_ws, size_t ws_size,
                              hipStream_t stream) {
  const unsigned short* x = (const unsigned short*)d_in[0];
  const unsigned short* kc = (const unsigned short*)d_in[1];
  const unsigned short* vc = (const unsigned short*)d_in[2];
  const unsigned short* wq = (const unsigned short*)d_in[3];
  const unsigned short* wo = (const unsigned short*)d_in[4];
  const unsigned short* wg = (const unsigned short*)d_in[6];
  const unsigned short* wu = (const unsigned short*)d_in[7];
  const unsigned short* wd = (const unsigned short*)d_in[8];
  const unsigned short* cv = (const unsigned short*)d_in[9];
  const unsigned short* wmod = (const unsigned short*)d_in[10];

  constexpr int WAVES = 8;
  float* ws = (float*)d_ws;
  float* mod = ws;
  float* x1 = mod + NB * NMOD;
  float* pout = x1 + NB * DM;
  float* p2 = pout + (size_t)NH * NB * DM;
  u64* qg = (u64*)(p2 + (size_t)112 * NB * DM);
  u64* og = qg + (size_t)NH * NB * DH;
  u64* xg = og + (size_t)NH * GRP * OREC;
  const int gran16 = (NH * NB * DH + NH * GRP * OREC + NB * DM) * 8 / 16;

  k_mod<<<96, 512, 0, stream>>>(wmod, cv, mod, (u32x4*)qg, gran16);
  k_attn<WAVES><<<NH * GRP, WAVES * 64, 0, stream>>>(x, kc, vc, wq, wo, mod, pout, qg, og);
  k_ffn<<<112, 512, 0, stream>>>(x, pout, xg, x1, mod, wg, wu, wd, p2);
  k_red2<<<NB * 8, 256, 0, stream>>>(x1, mod, p2, (__hip_bfloat16*)d_out);
}
```
